```python
import torch

def _softmax(x, axis=-1):
    return torch.softmax(x, dim=axis)
def _gelu(x):
    return torch.nn.functional.gelu(x, approximate="tanh")
def _silu(x):
    return torch.nn.functional.silu(x)
def _sigmoid(x):
    return torch.sigmoid(x)
def _layernorm(x, axis=-1, eps=1e-5):
    m = x.mean(dim=axis, keepdim=True)
    v = x.var(dim=axis, keepdim=True, unbiased=False)
    return (x - m) / torch.sqrt(v + eps)
def _scatter_add(tgt, idx, src, axis=0):
    return tgt.clone().index_add_(axis, idx, src)
def _segment_sum(src, seg, n):
    out = torch.zeros((n,) + src.shape[1:], dtype=src.dtype)
    return out.index_add_(0, seg, src)
def _pad(x, pw):
    flat = tuple(v for pair in reversed(pw) for v in pair)
    return torch.nn.functional.pad(x, flat)

kernel_name = 'gated_mix__sliding_block__dense_activation_mish__prompt'

def setup_inputs():

    _w = (lambda a: (lambda t: t.to(torch.bfloat16) if t.is_floating_point() else t)(torch.as_tensor(a)))
    def _A_setup():
        B, S, D = 1, 2048, 512
        return {
            "x": _w((lambda *s: torch.randn(*s, dtype=torch.bfloat16) * 0.05)(B, S, D)),
            "wqkv": _w((lambda *s: torch.randn(*s, dtype=torch.bfloat16) * 0.8)(D, 3 * D)),
            "wo": _w((lambda *s: torch.randn(*s, dtype=torch.bfloat16) * 0.05)(D, D)),
        }
    _A_in = _A_setup()

    def _B_setup():
        B, S, D, F = 1, 2048, 512, 1376
        return {
            "x": _w((lambda *s: torch.randn(*s, dtype=torch.bfloat16) * 0.05)(B, S, D)),
            "w1": _w((lambda *s: torch.randn(*s, dtype=torch.bfloat16) * 12.8)(D, F) * (D ** -0.5)),
            "b1": _w((lambda *s: torch.randn(*s, dtype=torch.bfloat16) * 0.8)(F)),
            "w2": _w((lambda *s: torch.randn(*s, dtype=torch.bfloat16) * 0.8)(F, D) * (F ** -0.5)),
        }
    _B_in = _B_setup()

    return {
        "A_x": _A_in["x"],
        "A_wqkv": _A_in["wqkv"],
        "A_wo": _A_in["wo"],
        "B_x": _B_in["x"],
        "B_w1": _B_in["w1"],
        "B_b1": _B_in["b1"],
        "B_w2": _B_in["w2"],
        "gate_w": _w((lambda *s: torch.randn(*s, dtype=torch.bfloat16) * 0.05)(512) * 16.0),
    }

def reference(A_x, A_wqkv, A_wo, B_x, B_w1, B_b1, B_w2, gate_w):
    def _A_ref(x, wqkv, wo):
        B, S, D = x.shape
        H = 32; Dh = D // H; W = 256
        qkv = (lambda x, *d: x.permute(*d))((x @ wqkv).reshape(B, S, 3, H, Dh), 2, 0, 3, 1, 4)
        q, k, v = qkv[0], qkv[1], qkv[2]
        scores = torch.einsum("bhqd,bhkd->bhqk", q, k) * (Dh ** -0.5)
        idx = torch.arange(S)
        mask = (idx[:, None] - idx[None, :] >= 0) & (idx[:, None] - idx[None, :] < W)
        scores = torch.where(mask[None, None], scores, -1e9)
        p = _softmax(scores, axis=-1)
        o = torch.einsum("bhqk,bhkd->bhqd", p, v)
        return (lambda x, *d: x.permute(*d))(o, 0, 2, 1, 3).reshape(B, S, D) @ wo

    def _B_ref(x, w1, b1, w2):
        h = torch.einsum("bsd,df->bsf", x, w1) + b1
        sp = torch.log1p(torch.exp(-torch.abs(h))) + torch.where(h > 0, h, torch.zeros_like(h))
        act = h * torch.tanh(sp)
        return torch.einsum("bsf,fd->bsd", act, w2)

    x = A_x
    g = _sigmoid((x * gate_w).sum(-1, keepdims=True))
    a = _A_ref(x, A_wqkv, A_wo)
    b = _B_ref(x, B_w1, B_b1, B_w2)
    return g * a + (1 - g) * b
```

```cpp
#include <hip/hip_runtime.h>
#include <hip/hip_bf16.h>
#include <stdint.h>

typedef __attribute__((ext_vector_type(8))) __bf16 bf16x8;
typedef __attribute__((ext_vector_type(4))) __bf16 bf16x4;
typedef __attribute__((ext_vector_type(2))) __bf16 bf16x2;
typedef __attribute__((ext_vector_type(4))) float f32x4;
typedef __attribute__((ext_vector_type(16))) float f32x16;
typedef __attribute__((ext_vector_type(2))) float f32x2;
typedef __attribute__((ext_vector_type(2))) unsigned u32x2;
typedef __attribute__((ext_vector_type(4))) unsigned u32x4;
#define LDSP __attribute__((address_space(3)))
#define DEV __device__ __forceinline__

constexpr int S_ = 2048, D_ = 512, F_ = 1376;
constexpr size_t BLK = 65536;
constexpr size_t WS_Q = 0, WS_K = 32 * BLK, WS_V = 64 * BLK, WS_ACT = 96 * BLK, WS_O = WS_ACT + 86 * BLK,
                 WS_G = WS_O + 32 * BLK;

DEV void glds16(const char* sbase, unsigned voff, unsigned lds_base) {
  asm volatile("s_mov_b32 m0, %0\n\ts_nop 0\n\tglobal_load_lds_dwordx4 %1, %2"
               :: "s"(lds_base), "v"(voff), "s"(sbase) : "memory");
}
#define WAIT_V(n) asm volatile("s_waitcnt vmcnt(" #n ")" ::: "memory")
#define BAR() do { asm volatile("s_waitcnt lgkmcnt(0)" ::: "memory"); __builtin_amdgcn_s_barrier(); asm volatile("" ::: "memory"); } while (0)

DEV bf16x8 lds_read16(const char* p) { return *(const LDSP bf16x8*)(const LDSP char*)p; }
DEV bf16x8 lds_tr2(unsigned a0, unsigned a1) {
  bf16x4 lo = __builtin_amdgcn_ds_read_tr16_b64_v4bf16((LDSP bf16x4*)(size_t)a0);
  bf16x4 hi = __builtin_amdgcn_ds_read_tr16_b64_v4bf16((LDSP bf16x4*)(size_t)a1);
  return __builtin_shufflevector(lo, hi, 0, 1, 2, 3, 4, 5, 6, 7);
}
DEV bf16x8 lds_rd(unsigned a) { return *(const LDSP bf16x8*)(size_t)a; }
DEV unsigned pk2(float a, float b) {
  f32x2 p = {a, b};
  bf16x2 q = __builtin_convertvector(p, bf16x2);
  return __builtin_bit_cast(unsigned, q);
}
DEV float bf2f(unsigned short u) { return __builtin_bit_cast(float, (unsigned)u << 16); }
DEV float mish(float h) {
  float t = __builtin_amdgcn_exp2f(-fabsf(h) * 1.4426950408889634f);
  float tt = t * t, t2 = t + t;
  float num = h > 0.f ? 1.f + t2 : tt + t2;
  float den = h > 0.f ? num + tt + tt : num + 2.f;
  return h * num * __builtin_amdgcn_rcpf(den);
}

template <int NS, bool FFN>
DEV void k1_body(char* smem, const char* x, const char* W, int mt, int nt, char* ws, const unsigned short* b1,
                 const unsigned short* gate_w) {
  constexpr int RB = 64 * NS, CPR = 4 * NS, LDW = FFN ? F_ * 2 : 3 * D_ * 2, NTOT = FFN ? F_ : 3 * D_;
  constexpr int STA = 16384, STB = 64 * RB, ST = STA + STB, NB = (8 * CPR + 63) / 64;
  const int tid = threadIdx.x, lane = tid & 63, w = __builtin_amdgcn_readfirstlane(tid >> 6);
  const int wm = w & 3, wn = w >> 2;
  const int m0 = mt * 128, n0 = nt * (32 * NS);
  const unsigned sm = (unsigned)(size_t)(LDSP char*)smem;
  unsigned offA[2], offB[NB];
#pragma unroll
  for (int i = 0; i < 2; ++i)
    offA[i] = (unsigned)((m0 + w * 16 + i * 8 + (lane >> 3)) * 1024 + (((lane & 7) ^ (lane >> 3)) << 4));
#pragma unroll
  for (int i = 0; i < NB; ++i) {
    int p = i * 64 + lane; p = p < 8 * CPR ? p : 0;
    int row = 8 * w + p / CPR, c = (p % CPR) ^ ((w & 1) << 1);
    int col = n0 + c * 8; col = col + 8 <= NTOT ? col : 0;
    offB[i] = (unsigned)(row * LDW + col * 2);
  }
  auto stage = [&](int kt, int buf) {
    const char* ga = x + kt * 128;
    const char* gb = W + (size_t)kt * 64 * LDW;
    unsigned la = sm + buf * ST + w * 2048, lb = sm + buf * ST + STA + w * 8 * RB;
    glds16(ga, offA[0], la);
    glds16(ga, offA[1], la + 1024);
#pragma unroll
    for (int i = 0; i < NB; ++i) {
      if (i * 64 + 64 <= 8 * CPR) glds16(gb, offB[i], lb + i * 1024);
      else if (lane < 8 * CPR - i * 64) glds16(gb, offB[i], lb + i * 1024);
    }
  };
  f32x4 acc[2][NS];
#pragma unroll
  for (int a = 0; a < 2; ++a)
#pragma unroll
    for (int b = 0; b < NS; ++b) acc[a][b] = f32x4{0.f, 0.f, 0.f, 0.f};
  stage(0, 0);
  stage(1, 1);
  const int g = lane >> 4, q = (lane & 15) >> 2, p = lane & 3, r16 = lane & 15;
  unsigned ra[2][2];
#pragma unroll
  for (int ms = 0; ms < 2; ++ms)
#pragma unroll
    for (int ks = 0; ks < 2; ++ks) {
      int row = wm * 32 + ms * 16 + r16;
      ra[ms][ks] = sm + row * 128 + (((ks * 4 + g) ^ (row & 7)) << 4);
    }
  unsigned rb[2];
  {
    int j0 = wn * NS;
    unsigned base = sm + STA + (8 * g + q) * RB + 8 * (p & 1) + (p >> 1) * 16 + 32 * j0;
    int d = (g & 1) ? ((j0 & 1) ? -32 : 32) : 0;
    rb[0] = base + d; rb[1] = base - d;
  }
#pragma unroll
  for (int kt = 0; kt < 8; ++kt) {
    const int buf = kt % 3;
    if (kt < 7) { if constexpr (NB == 4) WAIT_V(6); else WAIT_V(5); } else WAIT_V(0);
    BAR();
    if (kt + 2 < 8) stage(kt + 2, (kt + 2) % 3);
#pragma unroll
    for (int ks = 0; ks < 2; ++ks) {
      bf16x8 xa[2], wb[NS];
#pragma unroll
      for (int ms = 0; ms < 2; ++ms) xa[ms] = lds_rd(ra[ms][ks] + buf * ST);
#pragma unroll
      for (int ns = 0; ns < NS; ++ns) {
        unsigned a = rb[ns & 1] + buf * ST + 32 * ns + ks * 32 * RB;
        wb[ns] = lds_tr2(a, a + 4 * RB);
      }
#pragma unroll
      for (int ns = 0; ns < NS; ++ns)
#pragma unroll
        for (int ms = 0; ms < 2; ++ms)
          acc[ms][ns] = __builtin_amdgcn_mfma_f32_16x16x32_bf16(wb[ns], xa[ms], acc[ms][ns], 0, 0, 0);
    }
  }
#pragma unroll
  for (int ns = 0; ns < NS; ++ns) {
    const int js = nt * (2 * NS) + wn * NS + ns;
    if (js >= NTOT / 16) continue;
    float bias[4] = {0.f, 0.f, 0.f, 0.f};
    if constexpr (FFN) {
      u32x2 bb = *(const u32x2*)(b1 + js * 16 + 4 * g);
      bias[0] = __builtin_bit_cast(float, bb[0] << 16); bias[1] = __builtin_bit_cast(float, bb[0] & 0xffff0000u);
      bias[2] = __builtin_bit_cast(float, bb[1] << 16); bias[3] = __builtin_bit_cast(float, bb[1] & 0xffff0000u);
    }
#pragma unroll
    for (int ms = 0; ms < 2; ++ms) {
      const int m = m0 + wm * 32 + ms * 16 + r16;
      f32x4 v = acc[ms][ns];
      size_t off;
      if constexpr (FFN) {
#pragma unroll
        for (int r = 0; r < 4; ++r) v[r] = mish(v[r] + bias[r]);
        off = WS_ACT + (size_t)js * BLK + m * 32 + g * 8;
      } else {
        off = (size_t)js * BLK + (js < 64 ? (m >> 5) * 1024 + (g >> 1) * 512 + (m & 31) * 16 + (g & 1) * 8 : m * 32 + g * 8);
      }
      u32x2 o = {pk2(v[0], v[1]), pk2(v[2], v[3])};
      *(u32x2*)(ws + off) = o;
    }
  }
  if constexpr (FFN) {
    if (nt == 8) {
      const int m = m0 + w * 16 + (lane >> 2);
      float s = 0.f;
#pragma unroll 4
      for (int i = 0; i < 16; ++i) {
        const int k = (i * 4 + (lane & 3)) * 8;
        u32x4 xv = *(const u32x4*)(x + (size_t)m * 1024 + k * 2);
        u32x4 gv = *(const u32x4*)(gate_w + k);
#pragma unroll
        for (int j = 0; j < 4; ++j) {
          s += __builtin_bit_cast(float, xv[j] << 16) * __builtin_bit_cast(float, gv[j] << 16);
          s += __builtin_bit_cast(float, xv[j] & 0xffff0000u) * __builtin_bit_cast(float, gv[j] & 0xffff0000u);
        }
      }
      s += __shfl_xor(s, 1);
      s += __shfl_xor(s, 2);
      if ((lane & 3) == 0) ((float*)(ws + WS_G))[m] = 1.f / (1.f + __expf(-s));
    }
  }
}

__global__ __launch_bounds__(512, 2) void k1_proj(const char* __restrict__ x, const char* __restrict__ wqkv,
                                                  const char* __restrict__ w1, const unsigned short* __restrict__ b1,
                                                  const unsigned short* __restrict__ gate_w, char* __restrict__ ws) {
  __shared__ __attribute__((aligned(1024))) char smem[3 * (16384 + 64 * 448)];
  const int b = blockIdx.x, xc = b & 7, i = b >> 3;
  const int mt = (xc & 1) * 8 + (i & 7), nt = (xc >> 1) * 4 + (i >> 3);
  if (nt < 7) k1_body<7, false>(smem, x, wqkv, mt, nt, ws, b1, gate_w);
  else k1_body<5, true>(smem, x, w1, mt, nt - 7, ws, b1, gate_w);
}

__global__ __launch_bounds__(512, 2) void k2_attn(char* __restrict__ ws) {
  __shared__ __attribute__((aligned(1024))) char smem[16384 + 32768];
  const int tid = threadIdx.x, lane = tid & 63, w = __builtin_amdgcn_readfirstlane(tid >> 6);
  const int b = blockIdx.x, h = (b & 7) * 4 + ((b >> 3) & 3), qb = b >> 5;
  const unsigned sm = (unsigned)(size_t)(LDSP char*)smem;
  const char* Qg = ws + WS_Q + (size_t)h * BLK;
  const char* Kg = ws + WS_K + (size_t)h * BLK;
  const char* Vg = ws + WS_V + (size_t)h * BLK;
#pragma unroll
  for (int i = 0; i < 2; ++i) {
    int kt = qb * 8 - 8 + 2 * w + i; kt = kt < 0 ? 0 : kt;
    glds16(Kg + kt * 1024, lane * 16, sm + (2 * w + i) * 1024);
  }
#pragma unroll
  for (int i = 0; i < 4; ++i) {
    int kt = qb * 8 - 8 + 2 * w + (i >> 1); kt = kt < 0 ? 0 : kt;
    if ((lane & 2) == 0)
      glds16(Vg + kt * 1024 + (i & 1) * 512, (lane >> 2) * 32 + (lane & 1) * 16, sm + 16384 + (4 * w + i) * 1024);
  }
  const int qt = qb * 8 + w;
  bf16x8 qf = *(const bf16x8*)(Qg + qt * 1024 + lane * 16);
  {
    u32x4 ones = {0x3f803f80u, 0x3f803f80u, 0x3f803f80u, 0x3f803f80u};
#pragma unroll
    for (int i = 0; i < 2; ++i) {
      int c = i * 512 + tid;
      *(LDSP u32x4*)(size_t)(sm + 16384 + (c >> 1) * 64 + 32 + (c & 1) * 16) = ones;
    }
  }
  WAIT_V(0);
  BAR();
  f32x16 s[9];
  const f32x16 zero = {0.f, 0.f, 0.f, 0.f, 0.f, 0.f, 0.f, 0.f, 0.f, 0.f, 0.f, 0.f, 0.f, 0.f, 0.f, 0.f};
#pragma unroll
  for (int i = 0; i < 9; ++i) {
    bf16x8 kf = lds_rd(sm + (w + i) * 1024 + lane * 16);
    s[i] = __builtin_amdgcn_mfma_f32_32x32x16_bf16(kf, qf, zero, 0, 0, 0);
  }
  const int ql = lane & 31, hh = lane >> 5;
  const float NEG = -__builtin_inff();
#pragma unroll
  for (int r = 0; r < 16; ++r) {
    const int kl = (r & 3) + 8 * (r >> 2) + 4 * hh;
    s[0][r] = kl > ql ? s[0][r] : NEG;
    s[8][r] = kl <= ql ? s[8][r] : NEG;
  }
  if (qt < 8) {
#pragma unroll
    for (int i = 0; i < 8; ++i)
      if (i < 8 - qt) {
#pragma unroll
        for (int r = 0; r < 16; ++r) s[i][r] = NEG;
      }
  }
  float mx = NEG;
#pragma unroll
  for (int i = 0; i < 9; ++i)
#pragma unroll
    for (int r = 0; r < 16; r += 2) mx = fmaxf(fmaxf(mx, s[i][r]), s[i][r + 1]);
  mx = fmaxf(mx, __shfl_xor(mx, 32));
  const float c = 0.25f * 1.4426950408889634f;
  const float mc = -mx * c;
  f32x16 o = zero;
  const int G = lane >> 4;
  const unsigned va = sm + 16384 + w * 2048 + (4 * hh + ((lane & 15) >> 2)) * 64 + (G & 1) * 32 + (lane & 3) * 8;
#pragma unroll
  for (int i = 0; i < 9; ++i) {
#pragma unroll
    for (int st = 0; st < 2; ++st) {
      u32x4 pw;
#pragma unroll
      for (int j = 0; j < 4; ++j) {
        float p0 = __builtin_amdgcn_exp2f(__builtin_fmaf(s[i][8 * st + 2 * j], c, mc));
        float p1 = __builtin_amdgcn_exp2f(__builtin_fmaf(s[i][8 * st + 2 * j + 1], c, mc));
        pw[j] = pk2(p0, p1);
      }
      unsigned a = va + i * 2048 + st * 1024;
      bf16x8 vf = lds_tr2(a, a + 512);
      o = __builtin_amdgcn_mfma_f32_32x32x16_bf16(vf, __builtin_bit_cast(bf16x8, pw), o, 0, 0, 0);
    }
  }
  const float inv = __builtin_amdgcn_rcpf(o[8]);
  char* og = ws + WS_O + (size_t)h * BLK + (size_t)(qt * 32 + ql) * 32 + hh * 8;
  u32x2 o0 = {pk2(o[0] * inv, o[1] * inv), pk2(o[2] * inv, o[3] * inv)};
  u32x2 o1 = {pk2(o[4] * inv, o[5] * inv), pk2(o[6] * inv, o[7] * inv)};
  *(u32x2*)og = o0;
  *(u32x2*)(og + 16) = o1;
}

__global__ __launch_bounds__(512, 2) void k3_out(const char* __restrict__ ws, const char* __restrict__ wo,
                                                 const char* __restrict__ w2, char* __restrict__ out) {
  constexpr int ST = 32768, NST = 4, NSTEP = 15;
  __shared__ __attribute__((aligned(1024))) char smem[NST * ST];
  const int tid = threadIdx.x, lane = tid & 63, w = __builtin_amdgcn_readfirstlane(tid >> 6);
  const int b = blockIdx.x, xc = b & 7, i = b >> 3;
  const int mt = xc * 4 + (i & 3), nt = i >> 2;
  const int m0 = mt * 64, n0 = nt * 64;
  const unsigned sm = (unsigned)(size_t)(LDSP char*)smem;
  const int lkh = w >> 2, lr = (w & 3) * 16;
  unsigned offA[2], offB[2];
#pragma unroll
  for (int j = 0; j < 2; ++j) {
    int row = lr + j * 8 + (lane >> 3);
    int c = (lane & 7) ^ (row & 7);
    offA[j] = (unsigned)((c >> 1) * BLK + (m0 + row) * 32 + (c & 1) * 16);
    int f = ((row >> 1) & 1) | (((row >> 3) & 1) << 1);
    int cb = (lane & 7) ^ (f << 1);
    offB[j] = (unsigned)(row * 1024 + n0 * 2 + cb * 16);
  }
  auto stage = [&](int step, int buf) {
    const char *ga, *gb;
    if (step < 4) {
      int kc = step * 2 + lkh;
      ga = ws + WS_O + (size_t)kc * 4 * BLK; gb = wo + (size_t)kc * 64 * 1024;
    } else {
      int kc = (step - 4) * 2 + lkh;
      int k0 = kc < 21 ? kc * 64 : F_ - 64;
      ga = ws + WS_ACT + (size_t)(k0 >> 4) * BLK; gb = w2 + (size_t)k0 * 1024;
    }
    unsigned l = sm + buf * ST + lkh * 16384 + lr * 128;
    glds16(ga, offA[0], l);
    glds16(ga, offA[1], l + 1024);
    glds16(gb, offB[0], l + 8192);
    glds16(gb, offB[1], l + 8192 + 1024);
  };
  const int kh = w >> 2, wm = w & 1, wn = (w >> 1) & 1;
  const int g = lane >> 4, q = (lane & 15) >> 2, p = lane & 3, r16 = lane & 15;
  unsigned ra[2][2], rb[2];
#pragma unroll
  for (int ms = 0; ms < 2; ++ms)
#pragma unroll
    for (int ks = 0; ks < 2; ++ks) {
      int row = wm * 32 + ms * 16 + r16;
      ra[ms][ks] = sm + kh * 16384 + row * 128 + (((ks * 4 + g) ^ (row & 7)) << 4);
    }
#pragma unroll
  for (int ns = 0; ns < 2; ++ns) {
    int row = 8 * g + q;
    int f = ((row >> 1) & 1) | (((row >> 3) & 1) << 1);
    int c = (2 * (wn * 2 + ns) + (p >> 1)) ^ (f << 1);
    rb[ns] = sm + kh * 16384 + 8192 + row * 128 + c * 16 + 8 * (p & 1);
  }
  f32x4 accA[2][2], accB[2][2];
#pragma unroll
  for (int a = 0; a < 2; ++a)
#pragma unroll
    for (int c = 0; c < 2; ++c) { accA[a][c] = f32x4{0.f, 0.f, 0.f, 0.f}; accB[a][c] = f32x4{0.f, 0.f, 0.f, 0.f}; }
  stage(0, 0); stage(1, 1); stage(2, 2);
  auto compute = [&](int buf, f32x4 (&acc)[2][2], int ks0) {
#pragma unroll
    for (int ks = 0; ks < 2; ++ks) {
      if (ks < ks0) continue;
      bf16x8 xa[2], wb[2];
#pragma unroll
      for (int ms = 0; ms < 2; ++ms) xa[ms] = lds_rd(ra[ms][ks] + buf * ST);
#pragma unroll
      for (int ns = 0; ns < 2; ++ns) { unsigned a = rb[ns] + buf * ST + ks * 4096; wb[ns] = lds_tr2(a, a + 512); }
#pragma unroll
      for (int ns = 0; ns < 2; ++ns)
#pragma unroll
        for (int ms = 0; ms < 2; ++ms)
          acc[ms][ns] = __builtin_amdgcn_mfma_f32_16x16x32_bf16(wb[ns], xa[ms], acc[ms][ns], 0, 0, 0);
    }
  };
#pragma unroll
  for (int step = 0; step < NSTEP; ++step) {
    if (step < NSTEP - 2) WAIT_V(8); else if (step == NSTEP - 2) WAIT_V(4); else WAIT_V(0);
    BAR();
    if (step + 3 < NSTEP) stage(step + 3, (step + 3) % NST);
    if (step < 4) compute(step % NST, accA, 0);
    else if (step < NSTEP - 1) compute(step % NST, accB, 0);
    else compute(step % NST, accB, kh);
  }
  const float* gp = (const float*)(ws + WS_G);
  f32x4 res[2][2];
#pragma unroll
  for (int ms = 0; ms < 2; ++ms) {
    const float gt = gp[m0 + wm * 32 + ms * 16 + r16];
#pragma unroll
    for (int ns = 0; ns < 2; ++ns) res[ms][ns] = accA[ms][ns] * gt + accB[ms][ns] * (1.f - gt);
  }
  BAR();
  const unsigned xa_ = sm + (w & 3) * 4096 + lane * 16;
  if (kh == 1) {
#pragma unroll
    for (int ms = 0; ms < 2; ++ms)
#pragma unroll
      for (int ns = 0; ns < 2; ++ns) *(LDSP f32x4*)(size_t)(xa_ + (ms * 2 + ns) * 1024) = res[ms][ns];
  }
  BAR();
  if (kh == 0) {
#pragma unroll
    for (int ms = 0; ms < 2; ++ms)
#pragma unroll
      for (int ns = 0; ns < 2; ++ns) {
        f32x4 v = res[ms][ns] + *(const LDSP f32x4*)(size_t)(xa_ + (ms * 2 + ns) * 1024);
        const int m = m0 + wm * 32 + ms * 16 + r16, n = n0 + wn * 32 + ns * 16 + 4 * g;
        u32x2 o = {pk2(v[0], v[1]), pk2(v[2], v[3])};
        *(u32x2*)(out + (size_t)m * 1024 + n * 2) = o;
      }
  }
}

extern "C" void kernel_launch(void* const* d_in, const int* in_sizes, int n_in, void* d_out, int out_size, void* d_ws,
                              size_t ws_size, hipStream_t stream) {
  const char* x = (const char*)d_in[0];
  const char* wqkv = (const char*)d_in[1];
  const char* wo = (const char*)d_in[2];
  const char* w1 = (const char*)d_in[4];
  const unsigned short* b1 = (const unsigned short*)d_in[5];
  const char* w2 = (const char*)d_in[6];
  const unsigned short* gate_w = (const unsigned short*)d_in[7];
  char* ws = (char*)d_ws;
  k1_proj<<<256, 512, 0, stream>>>(x, wqkv, w1, b1, gate_w, ws);
  k2_attn<<<256, 512, 0, stream>>>(ws);
  k3_out<<<256, 512, 0, stream>>>(ws, wo, w2, (char*)d_out);
}
```

```cpp
#include <hip/hip_runtime.h>
#include <hip/hip_bf16.h>
#include <stdint.h>

typedef __attribute__((ext_vector_type(8))) __bf16 bf16x8;
typedef __attribute__((ext_vector_type(4))) __bf16 bf16x4;
typedef __attribute__((ext_vector_type(2))) __bf16 bf16x2;
typedef __attribute__((ext_vector_type(4))) float f32x4;
typedef __attribute__((ext_vector_type(16))) float f32x16;
typedef __attribute__((ext_vector_type(2))) float f32x2;
typedef __attribute__((ext_vector_type(2))) unsigned u32x2;
typedef __attribute__((ext_vector_type(4))) unsigned u32x4;
#define LDSP __attribute__((address_space(3)))
#define DEV __device__ __forceinline__

constexpr int S_ = 2048, D_ = 512, F_ = 1376;
constexpr size_t BLK = 65536;
constexpr size_t WS_Q = 0, WS_K = 32 * BLK, WS_V = 64 * BLK, WS_ACT = 96 * BLK, WS_O = WS_ACT + 86 * BLK,
                 WS_G = WS_O + 32 * BLK, WS_CNT = WS_G + 8192;
#define STMODE 1

DEV void glds16(const char* sbase, unsigned voff, unsigned lds_base) {
  asm volatile("s_mov_b32 m0, %0\n\ts_nop 0\n\tglobal_load_lds_dwordx4 %1, %2"
               :: "s"(lds_base), "v"(voff), "s"(sbase) : "memory");
}
#define STAMP(kid, i) do {} while (0)
#define WAIT_V(n) asm volatile("s_waitcnt vmcnt(" #n ")" ::: "memory")
#define BAR() do { asm volatile("s_waitcnt lgkmcnt(0)" ::: "memory"); __builtin_amdgcn_s_barrier(); asm volatile("" ::: "memory"); } while (0)

DEV bf16x8 lds_read16(const char* p) { return *(const LDSP bf16x8*)(const LDSP char*)p; }
DEV bf16x8 lds_tr2(unsigned a0, unsigned a1) {
  bf16x4 lo = __builtin_amdgcn_ds_read_tr16_b64_v4bf16((LDSP bf16x4*)(size_t)a0);
  bf16x4 hi = __builtin_amdgcn_ds_read_tr16_b64_v4bf16((LDSP bf16x4*)(size_t)a1);
  return __builtin_shufflevector(lo, hi, 0, 1, 2, 3, 4, 5, 6, 7);
}
DEV bf16x8 lds_rd(unsigned a) { return *(const LDSP bf16x8*)(size_t)a; }
DEV unsigned pk2(float a, float b) {
  f32x2 p = {a, b};
  bf16x2 q = __builtin_convertvector(p, bf16x2);
  return __builtin_bit_cast(unsigned, q);
}
DEV float bf2f(unsigned short u) { return __builtin_bit_cast(float, (unsigned)u << 16); }
DEV float mish(float h) {
  float e = __builtin_amdgcn_exp2f(fminf(h * 1.4426950408889634f, 30.f));
  float n = e * (e + 2.f);
  return h * n * __builtin_amdgcn_rcpf(n + 2.f);
}
DEV void swap16(unsigned& a, unsigned& b) {
  auto r = __builtin_amdgcn_permlane16_swap(a, b, false, false);
  a = r[0]; b = r[1];
}
DEV void swap32(unsigned& a, unsigned& b) {
  auto r = __builtin_amdgcn_permlane32_swap(a, b, false, false);
  a = r[0]; b = r[1];
}
DEV void st16(char* p, u32x4 v) {
  asm volatile("global_store_dwordx4 %0, %1, off sc1" :: "v"(p), "v"(v) : "memory");
}

template <int NS, bool FFN>
DEV void k1_body(char* smem, const char* x, const char* W, int mt, int nt, int ntg, char* ws, const unsigned short* b1,
                 const unsigned short* gate_w, const char* wo, const char* w2) {
  constexpr int RB = 64 * NS, CPR = 4 * NS, LDW = FFN ? F_ * 2 : 3 * D_ * 2, NTOT = FFN ? F_ : 3 * D_;
  constexpr int STA = 16384, STB = 64 * RB, ST = STA + STB;
  const int tid = threadIdx.x, lane = tid & 63, w = __builtin_amdgcn_readfirstlane(tid >> 6);
  const int m0 = mt * 128, n0 = nt * (32 * NS);
  const unsigned sm = (unsigned)(size_t)(LDSP char*)smem;
#define DIAGWS ws
  STAMP(0, 0);
  if (w >= 8) {
    const int pw = w - 8;
    unsigned offA[4], offB[NS];
#pragma unroll
    for (int i = 0; i < 4; ++i)
      offA[i] = (unsigned)((m0 + pw * 32 + i * 8 + (lane >> 3)) * 1024 + (((lane & 7) ^ (lane >> 3)) << 4));
#pragma unroll
    for (int i = 0; i < NS; ++i) {
      int p = i * 64 + lane;
      int row = 16 * pw + p / CPR, c = (p % CPR) ^ (((row >> 3) & 1) << 1);
      int col = n0 + c * 8; col = col + 8 <= NTOT ? col : 0;
      offB[i] = (unsigned)(row * LDW + col * 2);
    }
    const char* ga = x;
    const char* gb = W;
    unsigned lw = 0;
    auto stage = [&]() {
      unsigned la = sm + lw + pw * 4096, lb = sm + lw + STA + pw * 16 * RB;
#pragma unroll
      for (int i = 0; i < 4; ++i) glds16(ga, offA[i], la + i * 1024);
#pragma unroll
      for (int i = 0; i < NS; ++i) glds16(gb, offB[i], lb + i * 1024);
      ga += 128; gb += 64 * LDW;
      lw = lw == 2 * ST ? 0 : lw + ST;
    };
    stage();
    stage();
#pragma unroll 1
    for (int kt = 0; kt < 8; ++kt) {
      if (kt < 7) { if constexpr (NS == 7) WAIT_V(11); else WAIT_V(9); } else WAIT_V(0);
      BAR();
      if (kt < 6) stage();
    }
    {
      const int m = m0 + ntg * 8 + pw * 2 + (lane >> 5), k = (lane & 31) * 16;
      float s = 0.f;
#pragma unroll
      for (int i = 0; i < 2; ++i) {
        u32x4 xv = *(const u32x4*)(x + (size_t)m * 1024 + (k + i * 8) * 2);
        u32x4 gv = *(const u32x4*)(gate_w + k + i * 8);
#pragma unroll
        for (int j = 0; j < 4; ++j) {
          s += __builtin_bit_cast(float, xv[j] << 16) * __builtin_bit_cast(float, gv[j] << 16);
          s += __builtin_bit_cast(float, xv[j] & 0xffff0000u) * __builtin_bit_cast(float, gv[j] & 0xffff0000u);
        }
      }
#pragma unroll
      for (int d = 1; d < 32; d <<= 1) s += __shfl_xor(s, d);
      if ((lane & 31) == 0) ((float*)(ws + WS_G))[m] = 1.f / (1.f + __expf(-s));
      if (blockIdx.x == 0 && pw == 0 && lane < 8) ((unsigned*)(ws + WS_CNT))[lane * 32] = 0u;
      const int wid = blockIdx.x * 4 + pw;
      u32x4 t0, t1 = {0, 0, 0, 0}, t2 = {0, 0, 0, 0};
      asm volatile("global_load_dwordx4 %0, %1, %2" : "=v"(t0) : "v"((unsigned)(wid * 1376 + lane * 16)), "s"(w2) : "memory");
      if (lane < 22) asm volatile("global_load_dwordx4 %0, %1, %2" : "+v"(t1) : "v"((unsigned)(wid * 1376 + 1024 + lane * 16)), "s"(w2) : "memory");
      if (lane < 32) asm volatile("global_load_dwordx4 %0, %1, %2" : "+v"(t2) : "v"((unsigned)(wid * 512 + lane * 16)), "s"(wo) : "memory");
      WAIT_V(0);
      asm volatile("" :: "v"(t0), "v"(t1), "v"(t2));
    }
    return;
  }
  const int wm = w & 3, wn = w >> 2;
  f32x4 acc[2][NS];
#pragma unroll
  for (int a = 0; a < 2; ++a)
#pragma unroll
    for (int b = 0; b < NS; ++b) acc[a][b] = f32x4{0.f, 0.f, 0.f, 0.f};
  const int g = lane >> 4, q = (lane & 15) >> 2, p = lane & 3, r16 = lane & 15;
  unsigned ra[2], rb[2];
#pragma unroll
  for (int ks = 0; ks < 2; ++ks) {
    int row = wm * 32 + r16;
    ra[ks] = sm + row * 128 + (((ks * 4 + g) ^ (row & 7)) << 4);
  }
  {
    int j0 = wn * NS;
    unsigned base = sm + STA + (8 * g + q) * RB + 8 * (p & 1) + (p >> 1) * 16 + 32 * j0;
    int d = (g & 1) ? ((j0 & 1) ? -32 : 32) : 0;
    rb[0] = base + d; rb[1] = base - d;
  }
  unsigned lr = 0;
#pragma unroll 1
  for (int kt = 0; kt < 8; ++kt) {
    BAR();
    if (kt == 0) STAMP(0, 1);
    if (kt == 4) STAMP(0, 2);
#pragma unroll
    for (int ks = 0; ks < 2; ++ks) {
      bf16x8 xa[2], wb[NS];
#pragma unroll
      for (int ms = 0; ms < 2; ++ms) xa[ms] = lds_rd(ra[ks] + lr + ms * 2048);
#pragma unroll
      for (int ns = 0; ns < NS; ++ns) {
        unsigned a = rb[ns & 1] + lr + 32 * ns + ks * 32 * RB;
        wb[ns] = lds_tr2(a, a + 4 * RB);
      }
#pragma unroll
      for (int ns = 0; ns < NS; ++ns)
#pragma unroll
        for (int ms = 0; ms < 2; ++ms)
          acc[ms][ns] = __builtin_amdgcn_mfma_f32_16x16x32_bf16(wb[ns], xa[ms], acc[ms][ns], 0, 0, 0);
    }
    lr = lr == 2 * ST ? 0 : lr + ST;
  }
  STAMP(0, 3);
  const int m = m0 + wm * 32 + (g & 1) * 16 + r16;
#pragma unroll
  for (int ns = 0; ns < NS; ++ns) {
    const int js = nt * (2 * NS) + wn * NS + ns;
    if (js >= NTOT / 16) continue;
    f32x4 v0 = acc[0][ns], v1 = acc[1][ns];
    size_t off;
    if constexpr (FFN) {
      u32x2 bb = *(const u32x2*)(b1 + js * 16 + 4 * g);
      float bias[4];
      bias[0] = __builtin_bit_cast(float, bb[0] << 16); bias[1] = __builtin_bit_cast(float, bb[0] & 0xffff0000u);
      bias[2] = __builtin_bit_cast(float, bb[1] << 16); bias[3] = __builtin_bit_cast(float, bb[1] & 0xffff0000u);
#pragma unroll
      for (int r = 0; r < 4; ++r) { v0[r] = mish(v0[r] + bias[r]); v1[r] = mish(v1[r] + bias[r]); }
      off = WS_ACT + (size_t)js * BLK + m * 32 + (g >> 1) * 16;
    } else {
      off = (size_t)js * BLK + (js < 64 ? (m >> 5) * 1024 + (g >> 1) * 512 + (m & 31) * 16 : m * 32 + (g >> 1) * 16);
    }
    unsigned a0 = pk2(v0[0], v0[1]), a1 = pk2(v0[2], v0[3]), b0 = pk2(v1[0], v1[1]), b1_ = pk2(v1[2], v1[3]);
    swap16(a0, b0); swap16(a1, b1_);
    st16(ws + off, u32x4{a0, a1, b0, b1_});
  }
  STAMP(0, 4);
}

__global__ __launch_bounds__(768, 3) void k1_proj(const char* __restrict__ x, const char* __restrict__ wqkv,
                                                  const char* __restrict__ w1, const unsigned short* __restrict__ b1,
                                                  const unsigned short* __restrict__ gate_w, char* __restrict__ ws,
                                                  const char* __restrict__ wo, const char* __restrict__ w2) {
  __shared__ __attribute__((aligned(1024))) char smem[3 * (16384 + 64 * 448)];
  const int b = blockIdx.x, xc = b & 7, i = b >> 3;
  const int mt = (xc & 1) * 8 + (i & 7), nt = (xc >> 1) * 4 + (i >> 3);
  if (nt < 7) k1_body<7, false>(smem, x, wqkv, mt, nt, nt, ws, b1, gate_w, wo, w2);
  else k1_body<5, true>(smem, x, w1, mt, nt - 7, nt, ws, b1, gate_w, wo, w2);
}

typedef __attribute__((address_space(1))) unsigned gu32;
DEV void attn_block(char* smem, char* ws, int h, int qb) {
  const int tid = threadIdx.x, lane = tid & 63, w = __builtin_amdgcn_readfirstlane(tid >> 6);
  const unsigned sm = (unsigned)(size_t)(LDSP char*)smem;
#undef DIAGWS
#define DIAGWS ws
  STAMP(1, 0);
  const char* Qg = ws + WS_Q + (size_t)h * BLK;
  const char* Kg = ws + WS_K + (size_t)h * BLK;
  const char* Vg = ws + WS_V + (size_t)h * BLK;
#pragma unroll
  for (int i = 0; i < 2; ++i) {
    int kt = qb * 8 - 8 + 2 * w + i; kt = kt < 0 ? 0 : kt;
    glds16(Kg + kt * 1024, lane * 16, sm + (2 * w + i) * 1024);
  }
#pragma unroll
  for (int i = 0; i < 4; ++i) {
    int kt = qb * 8 - 8 + 2 * w + (i >> 1); kt = kt < 0 ? 0 : kt;
    if ((lane & 2) == 0)
      glds16(Vg + kt * 1024 + (i & 1) * 512, (lane >> 2) * 32 + (lane & 1) * 16, sm + 16384 + (4 * w + i) * 1024);
  }
  const int qt = qb * 8 + w;
  bf16x8 qf = *(const bf16x8*)(Qg + qt * 1024 + lane * 16);
  {
    u32x4 ones = {0x3f803f80u, 0x3f803f80u, 0x3f803f80u, 0x3f803f80u};
#pragma unroll
    for (int i = 0; i < 2; ++i) {
      int c = i * 512 + tid;
      *(LDSP u32x4*)(size_t)(sm + 16384 + (c >> 1) * 64 + 32 + (c & 1) * 16) = ones;
    }
  }
  WAIT_V(0);
  BAR();
  STAMP(1, 1);
  const f32x16 zero = {0.f, 0.f, 0.f, 0.f, 0.f, 0.f, 0.f, 0.f, 0.f, 0.f, 0.f, 0.f, 0.f, 0.f, 0.f, 0.f};
  const int ql = lane & 31, hh = lane >> 5;
  const float NEG = -__builtin_inff();
  const int lo = qt < 8 ? 8 - qt : 0;
  const unsigned ka = sm + w * 1024 + lane * 16;
  const int dq = ql - 4 * hh;
  auto score = [&](int i) {
    f32x16 s = __builtin_amdgcn_mfma_f32_32x32x16_bf16(lds_rd(ka + i * 1024), qf, zero, 0, 0, 0);
    if (i == 0) {
#pragma unroll
      for (int r = 0; r < 16; ++r) s[r] = (r & 3) + 8 * (r >> 2) > dq ? s[r] : NEG;
    }
    if (i == 8) {
#pragma unroll
      for (int r = 0; r < 16; ++r) s[r] = (r & 3) + 8 * (r >> 2) <= dq ? s[r] : NEG;
    }
    return s;
  };
  float mx = NEG;
#pragma unroll
  for (int i = 0; i < 9; ++i) {
    if (i < 8 && i < lo) continue;
    f32x16 s = score(i);
#pragma unroll
    for (int r = 0; r < 16; r += 2) mx = fmaxf(fmaxf(mx, s[r]), s[r + 1]);
  }
  mx = fmaxf(mx, __shfl_xor(mx, 32));
  STAMP(1, 2);
  const float c = 0.25f * 1.4426950408889634f;
  const float mc = -mx * c;
  f32x16 o = zero;
  const unsigned va = sm + 16384 + w * 2048 + (4 * hh + ((lane & 15) >> 2)) * 64 + ((lane >> 4) & 1) * 32 + (lane & 3) * 8;
#pragma unroll
  for (int i = 0; i < 9; ++i) {
    if (i < 8 && i < lo) continue;
    f32x16 s = score(i);
#pragma unroll
    for (int st = 0; st < 2; ++st) {
      u32x4 pw;
#pragma unroll
      for (int j = 0; j < 4; ++j) {
        float p0 = __builtin_amdgcn_exp2f(__builtin_fmaf(s[8 * st + 2 * j], c, mc));
        float p1 = __builtin_amdgcn_exp2f(__builtin_fmaf(s[8 * st + 2 * j + 1], c, mc));
        pw[j] = pk2(p0, p1);
      }
      unsigned a = va + i * 2048 + st * 1024;
      bf16x8 vf = lds_tr2(a, a + 512);
      o = __builtin_amdgcn_mfma_f32_32x32x16_bf16(vf, __builtin_bit_cast(bf16x8, pw), o, 0, 0, 0);
    }
  }
  STAMP(1, 3);
  const float inv = __builtin_amdgcn_rcpf(o[8]);
  unsigned a0 = pk2(o[0] * inv, o[1] * inv), a1 = pk2(o[2] * inv, o[3] * inv);
  unsigned b0 = pk2(o[4] * inv, o[5] * inv), b1 = pk2(o[6] * inv, o[7] * inv);
  swap32(a0, b0); swap32(a1, b1);
  char* og = ws + WS_O + (size_t)h * BLK + (size_t)(qt * 32 + ql) * 32 + hh * 16;
  asm volatile("global_store_dwordx4 %0, %1, off sc1" :: "v"(og), "v"(u32x4{a0, a1, b0, b1}) : "memory");
  WAIT_V(0);
  BAR();
  if (tid == 0) __hip_atomic_fetch_add((gu32*)(ws + WS_CNT) + qb * 32, 1u, __ATOMIC_RELAXED, __HIP_MEMORY_SCOPE_AGENT);
  STAMP(1, 4);
}

DEV void out_block(char* smem, const char* ws, const char* wo, const char* w2, char* out, int mt, int nt) {
  constexpr int ST = 16384, NST = 5, NSTEP = 30;
  const int tid = threadIdx.x, lane = tid & 63, w = __builtin_amdgcn_readfirstlane(tid >> 6);
  const int m0 = mt * 64, n0 = nt * 64;
  const unsigned sm = (unsigned)(size_t)(LDSP char*)smem;
#undef DIAGWS
#define DIAGWS ((char*)ws)
  STAMP(2, 0);
  if (w >= 6) return;
  if (w >= 4) {
    const int pw = w - 4;
    unsigned offA[4], offB[4];
#pragma unroll
    for (int j = 0; j < 4; ++j) {
      int row = pw * 32 + j * 8 + (lane >> 3);
      int c = (lane & 7) ^ (row & 7);
      offA[j] = (unsigned)((c >> 1) * BLK + (m0 + row) * 32 + (c & 1) * 16);
      int f = ((row >> 1) & 1) | (((row >> 3) & 1) << 1);
      int cb = (lane & 7) ^ (f << 1);
      offB[j] = (unsigned)(row * 1024 + n0 * 2 + cb * 16);
    }
    unsigned lw = 0;
    auto stage = [&](int step) {
      const char *ga, *gb;
      if (step < 22) {
        int k0 = step < 21 ? step * 64 : F_ - 64;
        ga = ws + WS_ACT + (size_t)(k0 >> 4) * BLK; gb = w2 + (size_t)k0 * 1024;
      } else {
        if (step == 22) {
          const gu32* cnt = (const gu32*)(ws + WS_CNT) + (mt >> 2) * 32;
          for (int it = 0; it < (1 << 22); ++it) {
            if (__hip_atomic_load(cnt, __ATOMIC_RELAXED, __HIP_MEMORY_SCOPE_AGENT) >= 32u) break;
            __builtin_amdgcn_s_sleep(2);
          }
          __builtin_amdgcn_fence(__ATOMIC_ACQUIRE, "agent");
        }
        ga = ws + WS_O + (size_t)(step - 22) * 4 * BLK; gb = wo + (size_t)(step - 22) * 65536;
      }
      unsigned l = sm + lw + pw * 4096;
#pragma unroll
      for (int j = 0; j < 4; ++j) glds16(ga, offA[j], l + j * 1024);
#pragma unroll
      for (int j = 0; j < 4; ++j) glds16(gb, offB[j], l + 8192 + j * 1024);
      lw = lw == (NST - 1) * ST ? 0 : lw + ST;
    };
#pragma unroll 1
    for (int s = 0; s < NST - 1; ++s) stage(s);
#pragma unroll 1
    for (int step = 0; step < NSTEP; ++step) {
      if (step <= NSTEP - 4) WAIT_V(24); else if (step == NSTEP - 3) WAIT_V(16); else if (step == NSTEP - 2) WAIT_V(8); else WAIT_V(0);
      BAR();
      if (step == 0) STAMP(2, 1);
      if (step + NST - 1 < NSTEP) stage(step + NST - 1);
    }
    return;
  }
  const int wm = w & 1, wn = w >> 1;
  const int g = lane >> 4, q = (lane & 15) >> 2, p = lane & 3, r16 = lane & 15;
  unsigned ra[2], rb[2];
#pragma unroll
  for (int ks = 0; ks < 2; ++ks) {
    int row = wm * 32 + r16;
    ra[ks] = sm + row * 128 + (((ks * 4 + g) ^ (row & 7)) << 4);
  }
#pragma unroll
  for (int ns = 0; ns < 2; ++ns) {
    int row = 8 * g + q;
    int f = ((row >> 1) & 1) | (((row >> 3) & 1) << 1);
    int c = (2 * (wn * 2 + ns) + (p >> 1)) ^ (f << 1);
    rb[ns] = sm + 8192 + row * 128 + c * 16 + 8 * (p & 1);
  }
  f32x4 acc[2][2], accB[2][2];
#pragma unroll
  for (int a = 0; a < 2; ++a)
#pragma unroll
    for (int c = 0; c < 2; ++c) acc[a][c] = f32x4{0.f, 0.f, 0.f, 0.f};
  const float* gp = (const float*)(ws + WS_G);
  float gt[2];
#pragma unroll
  for (int ms = 0; ms < 2; ++ms) gt[ms] = gp[m0 + wm * 32 + ms * 16 + r16];
  unsigned lr = 0;
#pragma unroll 1
  for (int step = 0; step < NSTEP; ++step) {
    BAR();
    if (step == 10) STAMP(2, 2);
    if (step == 22) {
#pragma unroll
      for (int a = 0; a < 2; ++a)
#pragma unroll
        for (int c = 0; c < 2; ++c) { accB[a][c] = acc[a][c]; acc[a][c] = f32x4{0.f, 0.f, 0.f, 0.f}; }
      STAMP(2, 3);
    }
#pragma unroll
    for (int ks = 0; ks < 2; ++ks) {
      if (ks == 0 && step == 21) continue;
      bf16x8 xa[2], wb[2];
#pragma unroll
      for (int ms = 0; ms < 2; ++ms) xa[ms] = lds_rd(ra[ks] + lr + ms * 2048);
#pragma unroll
      for (int ns = 0; ns < 2; ++ns) { unsigned a = rb[ns] + lr + ks * 4096; wb[ns] = lds_tr2(a, a + 512); }
#pragma unroll
      for (int ns = 0; ns < 2; ++ns)
#pragma unroll
        for (int ms = 0; ms < 2; ++ms)
          acc[ms][ns] = __builtin_amdgcn_mfma_f32_16x16x32_bf16(wb[ns], xa[ms], acc[ms][ns], 0, 0, 0);
    }
    lr = lr == (NST - 1) * ST ? 0 : lr + ST;
  }
  const int m = m0 + wm * 32 + (g & 1) * 16 + r16;
#pragma unroll
  for (int ns = 0; ns < 2; ++ns) {
    f32x4 v0 = acc[0][ns] * gt[0] + accB[0][ns] * (1.f - gt[0]);
    f32x4 v1 = acc[1][ns] * gt[1] + accB[1][ns] * (1.f - gt[1]);
    unsigned a0 = pk2(v0[0], v0[1]), a1 = pk2(v0[2], v0[3]), b0 = pk2(v1[0], v1[1]), b1 = pk2(v1[2], v1[3]);
    swap16(a0, b0); swap16(a1, b1);
    *(u32x4*)(out + (size_t)m * 1024 + (n0 + wn * 32 + ns * 16 + (g >> 1) * 8) * 2) = u32x4{a0, a1, b0, b1};
  }
  STAMP(2, 4);
}

__global__ __launch_bounds__(512, 4) void kb_attn_out(char* __restrict__ ws, const char* __restrict__ wo,
                                                      const char* __restrict__ w2, char* __restrict__ out) {
  __shared__ __attribute__((aligned(1024))) char smem[81920];
  const int b = blockIdx.x, xc = b & 7, i = b >> 3;
  const int type = (i ^ (i >> 5)) & 1, j = (i >> 5) * 16 + ((i & 31) >> 1);
  if (type == 0) attn_block(smem, ws, xc * 4 + (j & 3), j >> 2);
  else out_block(smem, ws, wo, w2, out, xc * 4 + (j & 3), j >> 2);
}

extern "C" void kernel_launch(void* const* d_in, const int* in_sizes, int n_in, void* d_out, int out_size, void* d_ws,
                              size_t ws_size, hipStream_t stream) {
  const char* x = (const char*)d_in[0];
  const char* wqkv = (const char*)d_in[1];
  const char* wo = (const char*)d_in[2];
  const char* w1 = (const char*)d_in[4];
  const unsigned short* b1 = (const unsigned short*)d_in[5];
  const char* w2 = (const char*)d_in[6];
  const unsigned short* gate_w = (const unsigned short*)d_in[7];
  char* ws = (char*)d_ws;
  k1_proj<<<256, 768, 0, stream>>>(x, wqkv, w1, b1, gate_w, ws, wo, w2);
  kb_attn_out<<<512, 512, 0, stream>>>(ws, wo, w2, (char*)d_out);
}
```

```cpp
#include <hip/hip_runtime.h>
#include <hip/hip_bf16.h>
#include <stdint.h>

typedef __attribute__((ext_vector_type(8))) __bf16 bf16x8;
typedef __attribute__((ext_vector_type(4))) __bf16 bf16x4;
typedef __attribute__((ext_vector_type(2))) __bf16 bf16x2;
typedef __attribute__((ext_vector_type(4))) float f32x4;
typedef __attribute__((ext_vector_type(16))) float f32x16;
typedef __attribute__((ext_vector_type(2))) float f32x2;
typedef __attribute__((ext_vector_type(2))) unsigned u32x2;
typedef __attribute__((ext_vector_type(4))) unsigned u32x4;
#define LDSP __attribute__((address_space(3)))
#define DEV __device__ __forceinline__

constexpr int S_ = 2048, D_ = 512, F_ = 1376;
constexpr int ACT_LD = 2816;
constexpr size_t BLK = 65536;
constexpr size_t WS_Q = 0, WS_K = 32 * BLK, WS_V = 64 * BLK, WS_ACT = 96 * BLK, WS_O = WS_ACT + 88 * BLK,
                 WS_G = WS_O + 32 * BLK, WS_CNT = WS_G + 8192;
#define STMODE 0

DEV void glds16(const char* sbase, unsigned voff, unsigned lds_base) {
  asm volatile("s_mov_b32 m0, %0\n\ts_nop 0\n\tglobal_load_lds_dwordx4 %1, %2"
               :: "s"(lds_base), "v"(voff), "s"(sbase) : "memory");
}
#define STAMP(kid, i) do {} while (0)
DEV void glds16_sc1(const char* sbase, unsigned voff, unsigned lds_base) {
  asm volatile("s_mov_b32 m0, %0\n\ts_nop 0\n\tglobal_load_lds_dwordx4 %1, %2 sc1"
               :: "s"(lds_base), "v"(voff), "s"(sbase) : "memory");
}
DEV void touch(unsigned& sink, const char* p) {
  asm volatile("global_load_dword %0, %1, off" : "+v"(sink) : "v"(p) : "memory");
}
#define WAIT_V(n) asm volatile("s_waitcnt vmcnt(" #n ")" ::: "memory")
#define BAR() do { asm volatile("s_waitcnt lgkmcnt(0)" ::: "memory"); __builtin_amdgcn_s_barrier(); asm volatile("" ::: "memory"); } while (0)

DEV bf16x8 lds_read16(const char* p) { return *(const LDSP bf16x8*)(const LDSP char*)p; }
DEV bf16x8 lds_tr2(unsigned a0, unsigned a1) {
  bf16x4 lo = __builtin_amdgcn_ds_read_tr16_b64_v4bf16((LDSP bf16x4*)(size_t)a0);
  bf16x4 hi = __builtin_amdgcn_ds_read_tr16_b64_v4bf16((LDSP bf16x4*)(size_t)a1);
  return __builtin_shufflevector(lo, hi, 0, 1, 2, 3, 4, 5, 6, 7);
}
DEV bf16x8 lds_rd(unsigned a) { return *(const LDSP bf16x8*)(size_t)a; }
DEV unsigned pk2(float a, float b) {
  f32x2 p = {a, b};
  bf16x2 q = __builtin_convertvector(p, bf16x2);
  return __builtin_bit_cast(unsigned, q);
}
DEV float bf2f(unsigned short u) { return __builtin_bit_cast(float, (unsigned)u << 16); }
DEV float mish(float h) {
  float e = __builtin_amdgcn_exp2f(fminf(h * 1.4426950408889634f, 30.f));
  float n = e * (e + 2.f);
  return h * n * __builtin_amdgcn_rcpf(n + 2.f);
}
DEV void swap16(unsigned& a, unsigned& b) {
  auto r = __builtin_amdgcn_permlane16_swap(a, b, false, false);
  a = r[0]; b = r[1];
}
DEV void swap32(unsigned& a, unsigned& b) {
  auto r = __builtin_amdgcn_permlane32_swap(a, b, false, false);
  a = r[0]; b = r[1];
}
DEV void st16(char* p, u32x4 v) {
  *(u32x4*)p = v;
}

template <int NS, bool FFN>
DEV void k1_body(char* smem, const char* x, const char* W, int mt, int nt, int ntg, char* ws, const unsigned short* b1,
                 const unsigned short* gate_w, const char* wo, const char* w2, const char* wq_, const char* w1_) {
  const char* const wall[2] = {wq_, w1_};
  constexpr int RB = 64 * NS, CPR = 4 * NS, LDW = FFN ? F_ * 2 : 3 * D_ * 2, NTOT = FFN ? F_ : 3 * D_;
  constexpr int STA = 16384, STB = 64 * RB, ST = STA + STB;
  const int tid = threadIdx.x, lane = tid & 63, w = __builtin_amdgcn_readfirstlane(tid >> 6);
  const int m0 = mt * 128, n0 = nt * (32 * NS);
  const unsigned sm = (unsigned)(size_t)(LDSP char*)smem;
#define DIAGWS ws
  STAMP(0, 0);
  if (w >= 8) {
    const int pw = w - 8;
    unsigned offA[4], offB[NS];
#pragma unroll
    for (int i = 0; i < 4; ++i)
      offA[i] = (unsigned)((m0 + pw * 32 + i * 8 + (lane >> 3)) * 1024 + (((lane & 7) ^ (lane >> 3)) << 4));
#pragma unroll
    for (int i = 0; i < NS; ++i) {
      int p = i * 64 + lane;
      int row = 16 * pw + p / CPR, c = (p % CPR) ^ (((row >> 3) & 1) << 1);
      int col = n0 + c * 8; col = col + 8 <= NTOT ? col : 0;
      offB[i] = (unsigned)(row * LDW + col * 2);
    }
    const char* ga = x;
    const char* gb = W;
    unsigned lw = 0;
    auto stage = [&]() {
      unsigned la = sm + lw + pw * 4096, lb = sm + lw + STA + pw * 16 * RB;
#pragma unroll
      for (int i = 0; i < 4; ++i) glds16(ga, offA[i], la + i * 1024);
#pragma unroll
      for (int i = 0; i < NS; ++i) glds16(gb, offB[i], lb + i * 1024);
      ga += 128; gb += 64 * LDW;
      lw = lw == 2 * ST ? 0 : lw + ST;
    };
    stage();
    stage();
#pragma unroll 1
    for (int kt = 0; kt < 8; ++kt) {
      if (kt < 7) { if constexpr (NS == 7) WAIT_V(11); else WAIT_V(9); } else WAIT_V(0);
      BAR();
      if (kt < 6) stage();
    }
    {
      const int m = m0 + ntg * 8 + pw * 2 + (lane >> 5), k = (lane & 31) * 16;
      float s = 0.f;
#pragma unroll
      for (int i = 0; i < 2; ++i) {
        u32x4 xv = *(const u32x4*)(x + (size_t)m * 1024 + (k + i * 8) * 2);
        u32x4 gv = *(const u32x4*)(gate_w + k + i * 8);
#pragma unroll
        for (int j = 0; j < 4; ++j) {
          s += __builtin_bit_cast(float, xv[j] << 16) * __builtin_bit_cast(float, gv[j] << 16);
          s += __builtin_bit_cast(float, xv[j] & 0xffff0000u) * __builtin_bit_cast(float, gv[j] & 0xffff0000u);
        }
      }
#pragma unroll
      for (int d = 1; d < 32; d <<= 1) s += __shfl_xor(s, d);
      if ((lane & 31) == 0) ((float*)(ws + WS_G))[m] = 1.f / (1.f + __expf(-s));
      if (blockIdx.x == 0 && pw == 0 && lane < 8) ((unsigned*)(ws + WS_CNT))[lane * 32] = 0u;
      const int wid = blockIdx.x * 4 + pw;
      u32x4 t0, t1 = {0, 0, 0, 0}, t2 = {0, 0, 0, 0};
      asm volatile("global_load_dwordx4 %0, %1, %2" : "=v"(t0) : "v"((unsigned)(wid * 1376 + lane * 16)), "s"(w2) : "memory");
      if (lane < 22) asm volatile("global_load_dwordx4 %0, %1, %2" : "+v"(t1) : "v"((unsigned)(wid * 1376 + 1024 + lane * 16)), "s"(w2) : "memory");
      if (lane < 32) asm volatile("global_load_dwordx4 %0, %1, %2" : "+v"(t2) : "v"((unsigned)(wid * 512 + lane * 16)), "s"(wo) : "memory");
      WAIT_V(0);
      asm volatile("" :: "v"(t0), "v"(t1), "v"(t2));
    }
    return;
  }
  const int wm = w & 3, wn = w >> 2;
  f32x4 acc[2][NS];
#pragma unroll
  for (int a = 0; a < 2; ++a)
#pragma unroll
    for (int b = 0; b < NS; ++b) acc[a][b] = f32x4{0.f, 0.f, 0.f, 0.f};
  const int g = lane >> 4, q = (lane & 15) >> 2, p = lane & 3, r16 = lane & 15;
  unsigned ra[2], rb[2];
#pragma unroll
  for (int ks = 0; ks < 2; ++ks) {
    int row = wm * 32 + r16;
    ra[ks] = sm + row * 128 + (((ks * 4 + g) ^ (row & 7)) << 4);
  }
  {
    int j0 = wn * NS;
    unsigned base = sm + STA + (8 * g + q) * RB + 8 * (p & 1) + (p >> 1) * 16 + 32 * j0;
    int d = (g & 1) ? ((j0 & 1) ? -32 : 32) : 0;
    rb[0] = base + d; rb[1] = base - d;
  }
  unsigned lr = 0;
#pragma unroll 1
  for (int kt = 0; kt < 8; ++kt) {
    BAR();
    if (kt == 0) STAMP(0, 1);
    if (kt == 4) STAMP(0, 2);
    bf16x8 xa[2][2], wb[2][NS];
#pragma unroll
    for (int ks = 0; ks < 2; ++ks) {
#pragma unroll
      for (int ms = 0; ms < 2; ++ms) xa[ks][ms] = lds_rd(ra[ks] + lr + ms * 2048);
#pragma unroll
      for (int ns = 0; ns < NS; ++ns) {
        unsigned a = rb[ns & 1] + lr + 32 * ns + ks * 32 * RB;
        wb[ks][ns] = lds_tr2(a, a + 4 * RB);
      }
    }
    __builtin_amdgcn_sched_barrier(0);
#pragma unroll
    for (int ks = 0; ks < 2; ++ks)
#pragma unroll
      for (int ns = 0; ns < NS; ++ns)
#pragma unroll
        for (int ms = 0; ms < 2; ++ms)
          acc[ms][ns] = __builtin_amdgcn_mfma_f32_16x16x32_bf16(wb[ks][ns], xa[ks][ms], acc[ms][ns], 0, 0, 0);
    lr = lr == 2 * ST ? 0 : lr + ST;
  }
  STAMP(0, 3);
#pragma unroll
  for (int ns = 0; ns < NS; ++ns) {
    const int js = nt * (2 * NS) + wn * NS + ns;
    if (js >= NTOT / 16) continue;
    float bias[4] = {0.f, 0.f, 0.f, 0.f};
    if constexpr (FFN) {
      u32x2 bb = *(const u32x2*)(b1 + js * 16 + 4 * g);
      bias[0] = __builtin_bit_cast(float, bb[0] << 16); bias[1] = __builtin_bit_cast(float, bb[0] & 0xffff0000u);
      bias[2] = __builtin_bit_cast(float, bb[1] << 16); bias[3] = __builtin_bit_cast(float, bb[1] & 0xffff0000u);
    }
#pragma unroll
    for (int mp = 0; mp < 1; ++mp) {
      const int m = m0 + wm * 32 + (g & 1) * 16 + r16;
      f32x4 v0 = acc[2 * mp][ns], v1 = acc[2 * mp + 1][ns];
      size_t off;
      if constexpr (FFN) {
#pragma unroll
        for (int r = 0; r < 4; ++r) { v0[r] = mish(v0[r] + bias[r]); v1[r] = mish(v1[r] + bias[r]); }
        off = WS_ACT + (size_t)m * ACT_LD + js * 32 + (g >> 1) * 16;
      } else {
        off = (size_t)js * BLK + (js < 64 ? (m >> 5) * 1024 + (g >> 1) * 512 + (m & 31) * 16 : m * 32 + (g >> 1) * 16);
      }
      unsigned a0 = pk2(v0[0], v0[1]), a1 = pk2(v0[2], v0[3]), b0 = pk2(v1[0], v1[1]), b1_ = pk2(v1[2], v1[3]);
      swap16(a0, b0); swap16(a1, b1_);
      st16(ws + off, u32x4{a0, a1, b0, b1_});
    }
  }
  STAMP(0, 4);
}

__global__ __launch_bounds__(768, 3) void k1_proj(const char* __restrict__ x, const char* __restrict__ wqkv,
                                                  const char* __restrict__ w1, const unsigned short* __restrict__ b1,
                                                  const unsigned short* __restrict__ gate_w, char* __restrict__ ws,
                                                  const char* __restrict__ wo, const char* __restrict__ w2) {
  __shared__ __attribute__((aligned(1024))) char smem[3 * (16384 + 64 * 448)];
  const int b = blockIdx.x, xc = b & 7, i = b >> 3;
  const int mt = (xc & 1) * 8 + (i & 7), nt = (xc >> 1) * 4 + (i >> 3);
  if (nt < 7) k1_body<7, false>(smem, x, wqkv, mt, nt, nt, ws, b1, gate_w, wo, w2, wqkv, w1);
  else k1_body<5, true>(smem, x, w1, mt, nt - 7, nt, ws, b1, gate_w, wo, w2, wqkv, w1);
}

typedef __attribute__((address_space(1))) unsigned gu32;
template <class F>
DEV void attn_block(char* smem, char* ws, int h, int qb, F&& after_stage) {
  const int tid = threadIdx.x, lane = tid & 63, w = __builtin_amdgcn_readfirstlane(tid >> 6);
  const unsigned sm = (unsigned)(size_t)(LDSP char*)smem;
#undef DIAGWS
#define DIAGWS ws
  STAMP(1, 0);
  __builtin_amdgcn_s_setprio(1);
  const char* Qg = ws + WS_Q + (size_t)h * BLK;
  const char* Kg = ws + WS_K + (size_t)h * BLK;
  const char* Vg = ws + WS_V + (size_t)h * BLK;
#pragma unroll
  for (int i = 0; i < 2; ++i) {
    int kt = qb * 8 - 8 + 2 * w + i; kt = kt < 0 ? 0 : kt;
    glds16(Kg + kt * 1024, lane * 16, sm + (2 * w + i) * 1024);
  }
#pragma unroll
  for (int i = 0; i < 4; ++i) {
    int kt = qb * 8 - 8 + 2 * w + (i >> 1); kt = kt < 0 ? 0 : kt;
    if ((lane & 2) == 0)
      glds16(Vg + kt * 1024 + (i & 1) * 512, (lane >> 2) * 32 + (lane & 1) * 16, sm + 16384 + (4 * w + i) * 1024);
  }
  const int qt = qb * 8 + w;
  bf16x8 qf = *(const bf16x8*)(Qg + qt * 1024 + lane * 16);
  {
    u32x4 ones = {0x3f803f80u, 0x3f803f80u, 0x3f803f80u, 0x3f803f80u};
#pragma unroll
    for (int i = 0; i < 2; ++i) {
      int c = i * 512 + tid;
      *(LDSP u32x4*)(size_t)(sm + 16384 + (c >> 1) * 64 + 32 + (c & 1) * 16) = ones;
    }
  }
  WAIT_V(0);
  asm volatile("" :: "v"(qf));
  BAR();
  after_stage();
  STAMP(1, 1);
  const f32x16 zero = {0.f, 0.f, 0.f, 0.f, 0.f, 0.f, 0.f, 0.f, 0.f, 0.f, 0.f, 0.f, 0.f, 0.f, 0.f, 0.f};
  const int ql = lane & 31, hh = lane >> 5;
  const float NEG = -__builtin_inff();
  const int lo = qt < 8 ? 8 - qt : 0;
  const unsigned ka = sm + w * 1024 + lane * 16;
  const int dq = ql - 4 * hh;
  auto score = [&](int i) {
    f32x16 s = __builtin_amdgcn_mfma_f32_32x32x16_bf16(lds_rd(ka + i * 1024), qf, zero, 0, 0, 0);
    if (i == 0) {
#pragma unroll
      for (int r = 0; r < 16; ++r) s[r] = (r & 3) + 8 * (r >> 2) > dq ? s[r] : NEG;
    }
    if (i == 8) {
#pragma unroll
      for (int r = 0; r < 16; ++r) s[r] = (r & 3) + 8 * (r >> 2) <= dq ? s[r] : NEG;
    }
    return s;
  };
  float mx = NEG;
  f32x16 sc[9];
#pragma unroll
  for (int i = 0; i < 9; ++i) {
    sc[i] = zero;
    if (i < 8 && i < lo) continue;
    sc[i] = score(i);
#pragma unroll
    for (int r = 0; r < 16; r += 2) mx = fmaxf(fmaxf(mx, sc[i][r]), sc[i][r + 1]);
  }
  mx = fmaxf(mx, __shfl_xor(mx, 32));
  STAMP(1, 2);
  const float c = 0.25f * 1.4426950408889634f;
  const float mc = -mx * c;
  f32x16 o = zero;
  const unsigned va = sm + 16384 + w * 2048 + (4 * hh + ((lane & 15) >> 2)) * 64 + ((lane >> 4) & 1) * 32 + (lane & 3) * 8;
#pragma unroll
  for (int i = 0; i < 9; ++i) {
    if (i < 8 && i < lo) continue;
    const f32x16 s = sc[i];
#pragma unroll
    for (int st = 0; st < 2; ++st) {
      u32x4 pw;
#pragma unroll
      for (int j = 0; j < 4; ++j) {
        float p0 = __builtin_amdgcn_exp2f(__builtin_fmaf(s[8 * st + 2 * j], c, mc));
        float p1 = __builtin_amdgcn_exp2f(__builtin_fmaf(s[8 * st + 2 * j + 1], c, mc));
        pw[j] = pk2(p0, p1);
      }
      unsigned a = va + i * 2048 + st * 1024;
      bf16x8 vf = lds_tr2(a, a + 512);
      o = __builtin_amdgcn_mfma_f32_32x32x16_bf16(vf, __builtin_bit_cast(bf16x8, pw), o, 0, 0, 0);
    }
  }
  STAMP(1, 3);
  const float inv = __builtin_amdgcn_rcpf(o[8]);
  unsigned a0 = pk2(o[0] * inv, o[1] * inv), a1 = pk2(o[2] * inv, o[3] * inv);
  unsigned b0 = pk2(o[4] * inv, o[5] * inv), b1 = pk2(o[6] * inv, o[7] * inv);
  swap32(a0, b0); swap32(a1, b1);
  char* og = ws + WS_O + (size_t)h * BLK + (size_t)(qt * 32 + ql) * 32 + hh * 16;
  asm volatile("global_store_dwordx4 %0, %1, off sc1" :: "v"(og), "v"(u32x4{a0, a1, b0, b1}) : "memory");
  STAMP(1, 4);
}

constexpr int OST = 16384, NST = 7, NSTEP = 30, RING = 49152;
struct OutProd {
  unsigned offA[2], offO[2], offB[2], lw, sm;
  const char *ws, *wo, *w2;
  int pw;
  DEV void init(char* smem, const char* ws_, const char* wo_, const char* w2_, int mt, int nt) {
    const int lane = threadIdx.x & 63;
    pw = __builtin_amdgcn_readfirstlane(threadIdx.x >> 6) - 4;
    ws = ws_; wo = wo_; w2 = w2_; lw = 0;
    sm = (unsigned)(size_t)(LDSP char*)smem + RING;
#pragma unroll
    for (int j = 0; j < 2; ++j) {
      int row = pw * 16 + j * 8 + (lane >> 3);
      int c = (lane & 7) ^ (row & 7);
      offA[j] = (unsigned)((mt * 64 + row) * ACT_LD + c * 16);
      offO[j] = (unsigned)((c >> 1) * BLK + (mt * 64 + row) * 32 + (c & 1) * 16);
      int f = ((row >> 1) & 1) | (((row >> 3) & 1) << 1);
      int cb = (lane & 7) ^ (f << 1);
      offB[j] = (unsigned)(row * 1024 + nt * 128 + cb * 16);
    }
  }
  DEV void stage(int step) {
    unsigned l = sm + lw + pw * 2048;
    const char* gb;
    if (step < 22) {
      int k0 = step < 21 ? step * 64 : F_ - 64;
      const char* ga = ws + WS_ACT + k0 * 2;
      gb = w2 + (size_t)k0 * 1024;
#pragma unroll
      for (int j = 0; j < 2; ++j) glds16(ga, offA[j], l + j * 1024);
    } else {
      const char* ga = ws + WS_O + (size_t)(step - 22) * 4 * BLK;
      gb = wo + (size_t)(step - 22) * 65536;
#pragma unroll
      for (int j = 0; j < 2; ++j) glds16_sc1(ga, offO[j], l + j * 1024);
    }
#pragma unroll
    for (int j = 0; j < 2; ++j) glds16(gb, offB[j], l + 8192 + j * 1024);
    lw = lw == (NST - 1) * OST ? 0 : lw + OST;
  }
};

DEV void out_block(char* smem, const char* ws, char* out, int mt, int nt, int qb, OutProd& P) {
  constexpr int ST = OST;
  const int tid = threadIdx.x, lane = tid & 63, w = __builtin_amdgcn_readfirstlane(tid >> 6);
  const int m0 = mt * 64, n0 = nt * 64;
  const unsigned sm = (unsigned)(size_t)(LDSP char*)smem + RING;
#undef DIAGWS
#define DIAGWS ((char*)ws)
  STAMP(2, 0);
  if (w >= 4) {
#pragma unroll 1
    for (int step = 0; step < NSTEP; ++step) {
      if (step <= NSTEP - 6) WAIT_V(16); else if (step == NSTEP - 5) WAIT_V(12); else if (step == NSTEP - 4) WAIT_V(8);
      else if (step == NSTEP - 3) WAIT_V(4); else WAIT_V(0);
      BAR();
      if (step + NST - 1 < NSTEP) P.stage(step + NST - 1);
    }
    return;
  }
  const int wm = w & 1, wn = w >> 1;
  const int g = lane >> 4, q = (lane & 15) >> 2, p = lane & 3, r16 = lane & 15;
  unsigned ra[2], rb[2];
#pragma unroll
  for (int ks = 0; ks < 2; ++ks) {
    int row = wm * 32 + r16;
    ra[ks] = sm + row * 128 + (((ks * 4 + g) ^ (row & 7)) << 4);
  }
#pragma unroll
  for (int ns = 0; ns < 2; ++ns) {
    int row = 8 * g + q;
    int f = ((row >> 1) & 1) | (((row >> 3) & 1) << 1);
    int c = (2 * (wn * 2 + ns) + (p >> 1)) ^ (f << 1);
    rb[ns] = sm + 8192 + row * 128 + c * 16 + 8 * (p & 1);
  }
  f32x4 acc[2][2], accB[2][2];
#pragma unroll
  for (int a = 0; a < 2; ++a)
#pragma unroll
    for (int c = 0; c < 2; ++c) acc[a][c] = f32x4{0.f, 0.f, 0.f, 0.f};
  const float* gp = (const float*)(ws + WS_G);
  float gt[2];
#pragma unroll
  for (int ms = 0; ms < 2; ++ms) gt[ms] = gp[m0 + wm * 32 + ms * 16 + r16];
  unsigned lr = 0;
  bf16x8 xa[2][2], wb[2][2];
  auto rdfrag = [&](bf16x8 (&x_)[2][2], bf16x8 (&w_)[2][2], unsigned off) {
#pragma unroll
    for (int ks = 0; ks < 2; ++ks) {
#pragma unroll
      for (int ms = 0; ms < 2; ++ms) x_[ks][ms] = lds_rd(ra[ks] + off + ms * 2048);
#pragma unroll
      for (int ns = 0; ns < 2; ++ns) { unsigned a = rb[ns] + off + ks * 4096; w_[ks][ns] = lds_tr2(a, a + 512); }
    }
  };
  const gu32* cnt = (const gu32*)(ws + WS_CNT) + (mt >> 2) * 32;
  unsigned seen = 0;
  bf16x8 xb[2][2], wc[2][2];
  auto dostep = [&](bf16x8 (&xc_)[2][2], bf16x8 (&wc_)[2][2], bf16x8 (&xn_)[2][2], bf16x8 (&wn_)[2][2], bool first, bool skip0) {
    BAR();
    if (first) rdfrag(xc_, wc_, 0);
    lr = lr == (NST - 1) * ST ? 0 : lr + ST;
    rdfrag(xn_, wn_, lr);
    __builtin_amdgcn_sched_barrier(0);
#pragma unroll
    for (int ks = 0; ks < 2; ++ks) {
      if (ks == 0 && skip0) continue;
#pragma unroll
      for (int ns = 0; ns < 2; ++ns)
#pragma unroll
        for (int ms = 0; ms < 2; ++ms)
          acc[ms][ns] = __builtin_amdgcn_mfma_f32_16x16x32_bf16(wc_[ks][ns], xc_[ks][ms], acc[ms][ns], 0, 0, 0);
    }
    __builtin_amdgcn_sched_barrier(0);
  };
  dostep(xa, wb, xb, wc, true, false);
  dostep(xb, wc, xa, wb, false, false);
#pragma unroll 1
  for (int it = 1; it < 11; ++it) {
    if (w == 0 && it == 8) {
      asm volatile("s_waitcnt vmcnt(0)" : "+v"(seen) :: "memory");
      if (seen < 32u) {
        for (int k = 0; k < (1 << 22); ++k) {
          if (__hip_atomic_load(cnt, __ATOMIC_RELAXED, __HIP_MEMORY_SCOPE_AGENT) >= 32u) break;
          __builtin_amdgcn_s_sleep(1);
        }
      }
    }
    if (it == 2) WAIT_V(0);
    dostep(xa, wb, xb, wc, false, false);
    if (it == 2 && tid == 0)
      __hip_atomic_fetch_add((gu32*)(const_cast<char*>(ws) + WS_CNT) + qb * 32, 1u, __ATOMIC_RELAXED, __HIP_MEMORY_SCOPE_AGENT);
    if (w == 0 && it == 6) asm volatile("global_load_dword %0, %1, off sc1" : "=v"(seen) : "v"(cnt) : "memory");
    if (it == 5) STAMP(2, 2);
    dostep(xb, wc, xa, wb, false, it == 10);
  }
#pragma unroll
  for (int a = 0; a < 2; ++a)
#pragma unroll
    for (int c = 0; c < 2; ++c) { accB[a][c] = acc[a][c]; acc[a][c] = f32x4{0.f, 0.f, 0.f, 0.f}; }
  STAMP(2, 3);
#pragma unroll 1
  for (int it = 0; it < 4; ++it) {
    dostep(xa, wb, xb, wc, false, false);
    dostep(xb, wc, xa, wb, false, false);
  }
  const int m = m0 + wm * 32 + (g & 1) * 16 + r16;
#pragma unroll
  for (int ns = 0; ns < 2; ++ns) {
    f32x4 v0 = acc[0][ns] * gt[0] + accB[0][ns] * (1.f - gt[0]);
    f32x4 v1 = acc[1][ns] * gt[1] + accB[1][ns] * (1.f - gt[1]);
    unsigned a0 = pk2(v0[0], v0[1]), a1 = pk2(v0[2], v0[3]), b0 = pk2(v1[0], v1[1]), b1 = pk2(v1[2], v1[3]);
    swap16(a0, b0); swap16(a1, b1);
    *(u32x4*)(out + (size_t)m * 1024 + (n0 + wn * 32 + ns * 16 + (g >> 1) * 8) * 2) = u32x4{a0, a1, b0, b1};
  }
  STAMP(2, 4);
}

__global__ __launch_bounds__(512, 2) void kb_attn_out(char* __restrict__ ws, const char* __restrict__ wo,
                                                      const char* __restrict__ w2, char* __restrict__ out) {
  __shared__ __attribute__((aligned(1024))) char smem[RING + NST * OST];
  const int b = blockIdx.x, xc = b & 7, j = b >> 3;
  const int mt = xc * 4 + (j & 3), nt = j >> 2;
  OutProd P;
  P.init(smem, ws, wo, w2, mt, nt);
  attn_block(smem, ws, xc * 4 + (j & 3), j >> 2, [&]() {
    if (P.pw >= 0) {
#pragma unroll 1
      for (int s = 0; s < NST - 1; ++s) P.stage(s);
    }
  });
  __builtin_amdgcn_s_setprio(0);
  out_block(smem, ws, out, mt, nt, j >> 2, P);
}

extern "C" void kernel_launch(void* const* d_in, const int* in_sizes, int n_in, void* d_out, int out_size, void* d_ws,
                              size_t ws_size, hipStream_t stream) {
  const char* x = (const char*)d_in[0];
  const char* wqkv = (const char*)d_in[1];
  const char* wo = (const char*)d_in[2];
  const char* w1 = (const char*)d_in[4];
  const unsigned short* b1 = (const unsigned short*)d_in[5];
  const char* w2 = (const char*)d_in[6];
  const unsigned short* gate_w = (const unsigned short*)d_in[7];
  char* ws = (char*)d_ws;
  k1_proj<<<256, 768, 0, stream>>>(x, wqkv, w1, b1, gate_w, ws, wo, w2);
  kb_attn_out<<<256, 512, 0, stream>>>(ws, wo, w2, (char*)d_out);
}
```
